```python
import torch

def _softmax(x, axis=-1):
    return torch.softmax(x, dim=axis)
def _gelu(x):
    return torch.nn.functional.gelu(x, approximate="tanh")
def _silu(x):
    return torch.nn.functional.silu(x)
def _sigmoid(x):
    return torch.sigmoid(x)
def _layernorm(x, axis=-1, eps=1e-5):
    m = x.mean(dim=axis, keepdim=True)
    v = x.var(dim=axis, keepdim=True, unbiased=False)
    return (x - m) / torch.sqrt(v + eps)
def _scatter_add(tgt, idx, src, axis=0):
    return tgt.clone().index_add_(axis, idx, src)
def _segment_sum(src, seg, n):
    out = torch.zeros((n,) + src.shape[1:], dtype=src.dtype)
    return out.index_add_(0, seg, src)
def _pad(x, pw):
    flat = tuple(v for pair in reversed(pw) for v in pair)
    return torch.nn.functional.pad(x, flat)

kernel_name = 'sequential_preLN__gqa_block_cached__geglu_ffn__decode'

def setup_inputs():

    _w = (lambda a: (lambda t: t.to(torch.bfloat16) if t.is_floating_point() else t)(torch.as_tensor(a)))
    def _A_setup():
        B, Sq, D = 4, 1, 512
        Hq, Hkv, Sk, Dh = 32, 32, 8192, 128
        assert Hq % Hkv == 0, (Hq, Hkv)
        return {
            "x": _w((lambda *s: torch.randn(*s, dtype=torch.bfloat16) * 0.05)(B, Sq, D)),
            "k_cache": _w((lambda *s: torch.randn(*s, dtype=torch.bfloat16) * 3.2)(B, Hkv, Sk, Dh)),
            "v_cache": _w((lambda *s: torch.randn(*s, dtype=torch.bfloat16) * 0.4)(B, Hkv, Sk, Dh)),
            "wq": _w((lambda *s: torch.randn(*s, dtype=torch.bfloat16) * 3.2)(D, Hq * Dh) * (D ** -0.5)),
            "wo": _w((lambda *s: torch.randn(*s, dtype=torch.bfloat16) * 0.4)(Hq * Dh, D) * ((Hq * Dh) ** -0.5)),
        }
    _A_in = _A_setup()

    def _B_setup():
        B, S, D, F = 4, 1, 512, 1376
        return {
            "x": _w((lambda *s: torch.randn(*s, dtype=torch.bfloat16) * 0.05)(B, S, D)),
            "w_gate": _w((lambda *s: torch.randn(*s, dtype=torch.bfloat16) * 0.05)(D, F)),
            "w_up": _w((lambda *s: torch.randn(*s, dtype=torch.bfloat16) * 0.05)(D, F)),
            "w_down": _w((lambda *s: torch.randn(*s, dtype=torch.bfloat16) * 0.05)(F, D)),
        }
    _B_in = _B_setup()

    return {
        "A_x": _A_in["x"],
        "A_k_cache": _A_in["k_cache"],
        "A_v_cache": _A_in["v_cache"],
        "A_wq": _A_in["wq"],
        "A_wo": _A_in["wo"],
        "B_x": _B_in["x"],
        "B_w_gate": _B_in["w_gate"],
        "B_w_up": _B_in["w_up"],
        "B_w_down": _B_in["w_down"],
    }

def reference(A_x, A_k_cache, A_v_cache, A_wq, A_wo, B_x, B_w_gate, B_w_up, B_w_down):
    def _A_ref(x, k_cache, v_cache, wq, wo):
        B, Sq, D = x.shape
        Hq, Dh = 32, 128
        Hkv = k_cache.shape[1]
        rep = Hq // Hkv
        q = (lambda x, *d: x.permute(*d))((x @ wq).reshape(B, Sq, Hq, Dh), 0, 2, 1, 3)
        k = (lambda x, n, axis: torch.repeat_interleave(x, n, dim=axis))(k_cache, rep, axis=1)
        v = (lambda x, n, axis: torch.repeat_interleave(x, n, dim=axis))(v_cache, rep, axis=1)
        scores = torch.einsum("bhqd,bhkd->bhqk", q, k) * (Dh ** -0.5)
        p = _softmax(scores, axis=-1)
        o = torch.einsum("bhqk,bhkd->bhqd", p, v)
        return (lambda x, *d: x.permute(*d))(o, 0, 2, 1, 3).reshape(B, Sq, Hq * Dh) @ wo

    def _B_ref(x, w_gate, w_up, w_down):
        return (_gelu(x @ w_gate) * (x @ w_up)) @ w_down

    x = A_x
    h = x + _A_ref(_layernorm(x, axis=-1), A_k_cache, A_v_cache, A_wq, A_wo)
    h2 = h + _B_ref(_layernorm(h, axis=-1), B_w_gate, B_w_up, B_w_down)
    return h2
```

```cpp
#include <hip/hip_runtime.h>
#include <hip/hip_bf16.h>
#include <stdint.h>

constexpr int B = 4, D = 512, H = 32, SK = 8192, DH = 128, F = 1376;
constexpr int NSPLIT = 2;
using bf16x2 = __attribute__((ext_vector_type(2))) __bf16;
using u32x4 = __attribute__((ext_vector_type(4))) unsigned;
using f32x4 = __attribute__((ext_vector_type(4))) float;
typedef unsigned short u16;

__device__ __forceinline__ float dot2(unsigned k, unsigned q, float acc) {
  return __builtin_amdgcn_fdot2_f32_bf16(__builtin_bit_cast(bf16x2, k), __builtin_bit_cast(bf16x2, q), acc, false);
}
__device__ __forceinline__ float bflo(unsigned x) { return __builtin_bit_cast(float, x << 16); }
__device__ __forceinline__ float bfhi(unsigned x) { return __builtin_bit_cast(float, x & 0xffff0000u); }
__device__ __forceinline__ float bf2f(u16 x) { return __builtin_bit_cast(float, (unsigned)x << 16); }
__device__ __forceinline__ unsigned cvtpk(float lo, float hi) {
  unsigned r; asm volatile("v_cvt_pk_bf16_f32 %0, %1, %2" : "=v"(r) : "v"(lo), "v"(hi)); return r;
}
__device__ __forceinline__ float rbf(float x) { return bflo(cvtpk(x, x)); }
template <int CTRL> __device__ __forceinline__ float dpp(float x) {
  return __builtin_bit_cast(float, __builtin_amdgcn_mov_dpp(__builtin_bit_cast(int, x), CTRL, 0xf, 0xf, true));
}
constexpr int XOR1 = 0xB1, XOR2 = 0x4E, XOR7 = 0x141, XOR8 = 0x128, ROR4 = 0x124;
__device__ __forceinline__ float xrow16_max(float x) {
  auto s = __builtin_amdgcn_permlane16_swap(__float_as_uint(x), __float_as_uint(x), false, false);
  x = fmaxf(__uint_as_float(s[0]), __uint_as_float(s[1]));
  auto t = __builtin_amdgcn_permlane32_swap(__float_as_uint(x), __float_as_uint(x), false, false);
  return fmaxf(__uint_as_float(t[0]), __uint_as_float(t[1]));
}
__device__ __forceinline__ float xrow16_sum(float x) {
  auto s = __builtin_amdgcn_permlane16_swap(__float_as_uint(x), __float_as_uint(x), false, false);
  x = __uint_as_float(s[0]) + __uint_as_float(s[1]);
  auto t = __builtin_amdgcn_permlane32_swap(__float_as_uint(x), __float_as_uint(x), false, false);
  return __uint_as_float(t[0]) + __uint_as_float(t[1]);
}
__device__ __forceinline__ float wave_sum(float t) {
  t += dpp<XOR1>(t); t += dpp<XOR2>(t); t += dpp<ROR4>(t); t += dpp<XOR8>(t); return xrow16_sum(t);
}
__device__ __forceinline__ float wave_max(float t) {
  t = fmaxf(t, dpp<XOR1>(t)); t = fmaxf(t, dpp<XOR2>(t)); t = fmaxf(t, dpp<ROR4>(t)); t = fmaxf(t, dpp<XOR8>(t)); return xrow16_max(t);
}


__global__ void __launch_bounds__(256) k_qproj(const u16* __restrict__ x, const u16* __restrict__ wq, u16* __restrict__ q) {
  __shared__ float ln[B][D];
  __shared__ float red[128][2][8][B];
  const int tid = threadIdx.x, lane = tid & 63, wave = tid >> 6;
  {
    const u32x4 xv = *reinterpret_cast<const u32x4*>(x + wave * D + lane * 8);
    float v[8];
    for (int k = 0; k < 4; ++k) { v[2 * k] = bflo(xv[k]); v[2 * k + 1] = bfhi(xv[k]); }
    float s = 0.f; for (int k = 0; k < 8; ++k) s += v[k];
    const float m = wave_sum(s) * (1.f / D);
    float ss = 0.f; for (int k = 0; k < 8; ++k) { const float d = v[k] - m; ss += d * d; }
    const float var = wave_sum(ss) * (1.f / D);
    const float rs = 1.f / sqrtf(var + 1e-5f);
    for (int k = 0; k < 8; ++k) ln[wave][lane * 8 + k] = rbf((v[k] - m) * rs);
  }
  __syncthreads();
  const int c0 = blockIdx.x * 16 + (tid & 1) * 8, rg = tid >> 1;
  float acc[B][8] = {};
  for (int i = 0; i < 4; ++i) {
    const int k = rg + 128 * i;
    const u32x4 w = *reinterpret_cast<const u32x4*>(wq + (size_t)k * (H * DH) + c0);
    float wf[8]; for (int j = 0; j < 4; ++j) { wf[2 * j] = bflo(w[j]); wf[2 * j + 1] = bfhi(w[j]); }
    for (int b = 0; b < B; ++b) { const float a = ln[b][k]; for (int j = 0; j < 8; ++j) acc[b][j] = fmaf(a, wf[j], acc[b][j]); }
  }
  for (int b = 0; b < B; ++b) for (int j = 0; j < 8; ++j) red[rg][tid & 1][j][b] = acc[b][j];
  __syncthreads();
  if (tid < 64) {
    const int b = tid & 3, j = (tid >> 2) & 7, half = tid >> 5;
    float s = 0.f; for (int r = 0; r < 128; ++r) s += red[r][half][j][b];
    const unsigned p = cvtpk(s, s);
    q[b * (H * DH) + blockIdx.x * 16 + half * 8 + j] = (u16)p;
  }
}

constexpr int AW = 8;
constexpr int TILE = 64;
__global__ void __launch_bounds__(AW * 64) k_attn(const u16* __restrict__ q, const u16* __restrict__ kc, const u16* __restrict__ vc,
                                                   float* __restrict__ part  ) {
  __shared__ float mrg[AW][DH + 4];
  const int tid = threadIdx.x, lane = tid & 63, wave = __builtin_amdgcn_readfirstlane(tid >> 6), r = lane >> 4, c = lane & 15;
  const int bh = blockIdx.x / NSPLIT, sp = blockIdx.x % NSPLIT;
  constexpr int KEYS = SK / NSPLIT, PAGES = KEYS / 16, PPW = PAGES / AW;
  constexpr float C2 = 0.088388347648318440f * 1.4426950408889634f;
  const u32x4 qp = *reinterpret_cast<const u32x4*>(q + (size_t)bh * DH + 8 * c);
  const u16* kb = kc + ((size_t)bh * SK + (size_t)sp * KEYS + wave * 16 + r) * DH + 8 * c;
  const u16* vb = vc + ((size_t)bh * SK + (size_t)sp * KEYS + wave * 16 + r) * DH + 8 * c;
  u32x4 Kn[4], Vn[4];
#define ISSUE(i_) do { _Pragma("unroll") for (int t_ = 0; t_ < 4; ++t_) { \
    Kn[t_] = __builtin_nontemporal_load(reinterpret_cast<const u32x4*>(kb + ((size_t)(i_) * AW * 16 + t_ * 4) * DH)); \
    Vn[t_] = __builtin_nontemporal_load(reinterpret_cast<const u32x4*>(vb + ((size_t)(i_) * AW * 16 + t_ * 4) * DH)); } } while (0)
  float m = -INFINITY, l = 0.f, acc[8] = {};
  ISSUE(0);
  for (int i = 0; i < PPW; ++i) {
    u32x4 Kc[4], Vc[4];
#pragma unroll
    for (int t = 0; t < 4; ++t) { Kc[t] = Kn[t]; Vc[t] = Vn[t]; }
    if (i + 1 < PPW) ISSUE(i + 1);
    float x[4];
#pragma unroll
    for (int t = 0; t < 4; ++t) { float d = 0.f;
#pragma unroll
      for (int k = 0; k < 4; ++k) d = dot2(Kc[t][k], qp[k], d);
      x[t] = d; }
#pragma unroll
    for (int t = 0; t < 4; ++t) { float v = x[t]; v += dpp<XOR1>(v); v += dpp<XOR2>(v); v += dpp<ROR4>(v); v += dpp<XOR8>(v); x[t] = v * C2; }
    float pm = fmaxf(fmaxf(x[0], x[1]), fmaxf(x[2], x[3]));
    pm = xrow16_max(pm);
    const float mn = fmaxf(m, pm);
    const float alpha = __builtin_amdgcn_exp2f(m - mn);
    m = mn;
    float ps = 0.f;
#pragma unroll
    for (int t = 0; t < 4; ++t) { x[t] = __builtin_amdgcn_exp2f(x[t] - mn); ps += x[t]; }
    l = fmaf(l, alpha, ps);
#pragma unroll
    for (int k = 0; k < 8; ++k) acc[k] *= alpha;
#pragma unroll
    for (int t = 0; t < 4; ++t)
#pragma unroll
      for (int k = 0; k < 4; ++k) { acc[2 * k] = fmaf(x[t], bflo(Vc[t][k]), acc[2 * k]); acc[2 * k + 1] = fmaf(x[t], bfhi(Vc[t][k]), acc[2 * k + 1]); }
  }
#undef ISSUE
#pragma unroll
  for (int k = 0; k < 8; ++k) acc[k] = xrow16_sum(acc[k]);
  l = xrow16_sum(l);
  if (r == 0) {
#pragma unroll
    for (int k = 0; k < 8; ++k) mrg[wave][4 + 8 * c + k] = acc[k];
    if (c == 0) { mrg[wave][0] = m; mrg[wave][1] = l; }
  }
  __syncthreads();
  if (tid < DH) {
    float M = -INFINITY;
    for (int w = 0; w < AW; ++w) M = fmaxf(M, mrg[w][0]);
    float L = 0.f, o = 0.f;
    for (int w = 0; w < AW; ++w) { const float wt = __builtin_amdgcn_exp2f(mrg[w][0] - M); L = fmaf(wt, mrg[w][1], L); o = fmaf(wt, mrg[w][4 + tid], o); }
    float* p = part + (size_t)blockIdx.x * (DH + 2);
    p[2 + tid] = o;
    if (tid == 0) { p[0] = M; p[1] = L; }
  }
}

__global__ void __launch_bounds__(256) k_wo(const float* __restrict__ part, const u16* __restrict__ wo, const u16* __restrict__ x, float* __restrict__ h) {
  __shared__ float o[B][H * DH];
  __shared__ float red[4][B][8];
  const int tid = threadIdx.x, lane = tid & 63, wave = tid >> 6;
  for (int i = tid; i < B * H * DH; i += 256) {
    const int bh = i / DH, d = i % DH;
    const float* p0 = part + (size_t)(bh * NSPLIT) * (DH + 2);
    float M = -INFINITY;
    for (int s = 0; s < NSPLIT; ++s) M = fmaxf(M, p0[s * (DH + 2)]);
    float L = 0.f, v = 0.f;
    for (int s = 0; s < NSPLIT; ++s) { const float w = __builtin_amdgcn_exp2f(p0[s * (DH + 2)] - M); L = fmaf(w, p0[s * (DH + 2) + 1], L); v = fmaf(w, p0[s * (DH + 2) + 2 + d], v); }
    o[bh / H][(bh % H) * DH + d] = rbf(v / L);
  }
  __syncthreads();
  const int c0 = blockIdx.x * 8;
  float acc[B][8] = {};
  for (int k = tid; k < H * DH; k += 256) {
    const u32x4 w = *reinterpret_cast<const u32x4*>(wo + (size_t)k * D + c0);
    float wf[8]; for (int j = 0; j < 4; ++j) { wf[2 * j] = bflo(w[j]); wf[2 * j + 1] = bfhi(w[j]); }
    for (int b = 0; b < B; ++b) { const float a = o[b][k]; for (int j = 0; j < 8; ++j) acc[b][j] = fmaf(a, wf[j], acc[b][j]); }
  }
  for (int b = 0; b < B; ++b) for (int j = 0; j < 8; ++j) { const float s = wave_sum(acc[b][j]); if (lane == 0) red[wave][b][j] = s; }
  __syncthreads();
  if (tid < 32) { const int b = tid >> 3, j = tid & 7;
    const float s = red[0][b][j] + red[1][b][j] + red[2][b][j] + red[3][b][j];
    h[b * D + c0 + j] = rbf(bf2f(x[b * D + c0 + j]) + rbf(s)); }
}

__global__ void __launch_bounds__(256) k_gu(const float* __restrict__ h, const u16* __restrict__ wg, const u16* __restrict__ wu, float* __restrict__ act) {
  __shared__ float ln[B][D];
  __shared__ float red[4][2][B][8];
  const int tid = threadIdx.x, lane = tid & 63, wave = tid >> 6;
  {
    float v[8]; for (int k = 0; k < 8; ++k) v[k] = h[wave * D + lane * 8 + k];
    float s = 0.f; for (int k = 0; k < 8; ++k) s += v[k];
    const float m = wave_sum(s) * (1.f / D);
    float ss = 0.f; for (int k = 0; k < 8; ++k) { const float d = v[k] - m; ss += d * d; }
    const float var = wave_sum(ss) * (1.f / D);
    const float rs = 1.f / sqrtf(var + 1e-5f);
    for (int k = 0; k < 8; ++k) ln[wave][lane * 8 + k] = rbf((v[k] - m) * rs);
  }
  __syncthreads();
  const int c0 = blockIdx.x * 8;
  float ag[B][8] = {}, au[B][8] = {};
  for (int k = tid; k < D; k += 256) {
    const u32x4 g = *reinterpret_cast<const u32x4*>(wg + (size_t)k * F + c0);
    const u32x4 u = *reinterpret_cast<const u32x4*>(wu + (size_t)k * F + c0);
    for (int b = 0; b < B; ++b) { const float a = ln[b][k];
      for (int j = 0; j < 4; ++j) { ag[b][2*j] = fmaf(a, bflo(g[j]), ag[b][2*j]); ag[b][2*j+1] = fmaf(a, bfhi(g[j]), ag[b][2*j+1]);
                                    au[b][2*j] = fmaf(a, bflo(u[j]), au[b][2*j]); au[b][2*j+1] = fmaf(a, bfhi(u[j]), au[b][2*j+1]); } }
  }
  for (int b = 0; b < B; ++b) for (int j = 0; j < 8; ++j) { const float s = wave_sum(ag[b][j]), t = wave_sum(au[b][j]); if (lane == 0) { red[wave][0][b][j] = s; red[wave][1][b][j] = t; } }
  __syncthreads();
  if (tid < 32) { const int b = tid >> 3, j = tid & 7;
    const float g = rbf(red[0][0][b][j] + red[1][0][b][j] + red[2][0][b][j] + red[3][0][b][j]);
    const float u = rbf(red[0][1][b][j] + red[1][1][b][j] + red[2][1][b][j] + red[3][1][b][j]);
    const float inner = 0.7978845608028654f * (g + 0.044715f * g * g * g);
    const float ge = rbf(0.5f * g * (1.f + tanhf(inner)));
    act[b * F + c0 + j] = rbf(ge * u); }
}

__global__ void __launch_bounds__(256) k_down(const float* __restrict__ act, const u16* __restrict__ wd, const float* __restrict__ h, u16* __restrict__ out) {
  __shared__ float red[4][B][8];
  const int tid = threadIdx.x, lane = tid & 63, wave = tid >> 6;
  const int c0 = blockIdx.x * 8;
  float acc[B][8] = {};
  for (int k = tid; k < F; k += 256) {
    const u32x4 w = *reinterpret_cast<const u32x4*>(wd + (size_t)k * D + c0);
    for (int b = 0; b < B; ++b) { const float a = act[b * F + k];
      for (int j = 0; j < 4; ++j) { acc[b][2*j] = fmaf(a, bflo(w[j]), acc[b][2*j]); acc[b][2*j+1] = fmaf(a, bfhi(w[j]), acc[b][2*j+1]); } }
  }
  for (int b = 0; b < B; ++b) for (int j = 0; j < 8; ++j) { const float s = wave_sum(acc[b][j]); if (lane == 0) red[wave][b][j] = s; }
  __syncthreads();
  if (tid < 32) { const int b = tid >> 3, j = tid & 7;
    const float s = red[0][b][j] + red[1][b][j] + red[2][b][j] + red[3][b][j];
    const float r = h[b * D + c0 + j] + rbf(s);
    out[b * D + c0 + j] = (u16)cvtpk(r, r); }
}

extern "C" void kernel_launch(void* const* d_in, const int* in_sizes, int n_in, void* d_out, int out_size, void* d_ws, size_t ws_size, hipStream_t stream) {
  const u16* x = (const u16*)d_in[0]; const u16* kc = (const u16*)d_in[1]; const u16* vc = (const u16*)d_in[2];
  const u16* wq = (const u16*)d_in[3]; const u16* wo = (const u16*)d_in[4];
  const u16* wg = (const u16*)d_in[6]; const u16* wu = (const u16*)d_in[7]; const u16* wd = (const u16*)d_in[8];
  char* ws = (char*)d_ws;
  u16* q = (u16*)ws;
  float* part = (float*)(ws + 65536);
  float* h = (float*)(ws + 65536 + 262144);
  float* act = (float*)(ws + 65536 + 262144 + 65536);
  k_qproj<<<256, 256, 0, stream>>>(x, wq, q);
  k_attn<<<B * H * NSPLIT, AW * 64, 0, stream>>>(q, kc, vc, part);
  k_wo<<<D / 8, 256, 0, stream>>>(part, wo, x, h);
  k_gu<<<F / 8, 256, 0, stream>>>(h, wg, wu, act);
  k_down<<<D / 8, 256, 0, stream>>>(act, wd, h, (u16*)d_out);
}
```

```cpp
#include <hip/hip_runtime.h>
#include <hip/hip_bf16.h>
#include <stdint.h>

namespace {

constexpr int NB = 4, DM = 512, NH = 32, SK = 8192, DH = 128, FF = 1376;
constexpr int NWG = 256, NTHR = 512, NW = 8;
#define CFG_P 24
#define CFG_HD 6
#define CFG_DP 6
#define CFG_RB 150
#define CFG_RB3 150
#define CFG_C0 112
#define CFG_CI 64
#define CFG_NCK 6
#define CFG_DL 350
#define CFG_Q0 41
#define CFG_QLO 34
#define CFG_QHI 48
#define CFG_HZ 28
#define CFG_FT 24
#define CFG_FR 3
#define CFG_GSL 2
#define CFG_OUTAUX 0
#define CFG_WZ0 0
constexpr int NS = 4;
constexpr int PRE = CFG_P;
constexpr int HD = CFG_HD;
constexpr int DEPTH0 = CFG_DP;
constexpr int RING = 8;
constexpr int FT = CFG_FT, FR = CFG_FR;
constexpr int TILE_KEYS = NS * 4, NT = (SK / 2) / TILE_KEYS, TILE_BYTES = TILE_KEYS * DH * 2;
static_assert(FT % RING == 0 && FT > RING && FT <= PRE && PRE % RING == 0 && PRE >= RING && HD < RING && DEPTH0 <= PRE && NT % RING == 0 && CFG_WZ0 + 16 < FT, "ring");
static_assert(CFG_C0 + (CFG_NCK - 1) * CFG_CI <= 2 * NT - FT - RING && CFG_NCK <= 16, "checkpoints");

constexpr unsigned WS_EPOCH = 0, WS_FLAGS = 4096, WS_BOX = 8192;
constexpr int HREP = 16, AREP = 4;
constexpr unsigned H_BYTES = NB * DM * 2, ACT_BYTES = 11264;
constexpr unsigned WS_Y = WS_BOX, WS_ML = WS_Y + NB * 16 * 64 * 128, WS_H = WS_ML + NWG * 128, WS_ACT = WS_H + HREP * H_BYTES, WS_PACE = WS_ACT + AREP * ACT_BYTES, WS_END = WS_PACE + 16 * 8 * 128;
constexpr unsigned CLR = (WS_END - WS_BOX + NWG * 16 - 1) / (NWG * 16);
static_assert(CLR > 128 && CLR <= 192 && NB * FF * 2 <= ACT_BYTES && WS_BOX + NWG * CLR * 16 <= (1u << 20), "boxes");
constexpr unsigned WS_BYTES = 1u << 20;
constexpr unsigned EMPTY = 0xFFFFFFFFu;
constexpr unsigned SPIN_CAP = 1u << 16;

constexpr unsigned L_W = 0;
constexpr unsigned L_Q = 65536;
constexpr unsigned L_RED = L_Q + 4096;
constexpr unsigned L_ML = L_RED + 4096;
constexpr unsigned L_PROG = L_ML + 32, L_MASK = L_ML + 48;
constexpr unsigned L_ST = L_ML + 64;
constexpr unsigned L_A = L_ML + 128;
constexpr unsigned L_R2 = L_A + 4096;
constexpr unsigned L_XN = L_R2 + 4096;
constexpr unsigned S_ROW = NT * 4 + 16;
constexpr unsigned L_S = L_XN + 4096;
constexpr unsigned L_BYTES = 106496;
static_assert(L_S + NS * 4 * S_ROW <= L_BYTES && L_S + NW * 2048 <= L_BYTES, "lds");

typedef unsigned short u16;
typedef unsigned long long u64;
using bf16x2 = __attribute__((ext_vector_type(2))) __bf16;
using bf16x8 = __attribute__((ext_vector_type(8))) __bf16;
using u32x4 = __attribute__((ext_vector_type(4))) unsigned;
using u32x2 = __attribute__((ext_vector_type(2))) unsigned;
using f32x4 = __attribute__((ext_vector_type(4))) float;
using f32x2 = __attribute__((ext_vector_type(2))) float;
using s16x4 = __attribute__((ext_vector_type(4))) short;
#define GAS __attribute__((address_space(1)))
#define LAS __attribute__((address_space(3)))
typedef __amdgpu_buffer_rsrc_t rsrc_t;
constexpr int AUX_NT = 2, AUX_SC1 = 16, AUX_POLL = 16, RSRC3 = 0x00020000;

__device__ __forceinline__ float dot2(unsigned k, unsigned q, float acc) {
  return __builtin_amdgcn_fdot2_f32_bf16(__builtin_bit_cast(bf16x2, k), __builtin_bit_cast(bf16x2, q), acc, false);
}
__device__ __forceinline__ float bflo(unsigned x) { return __builtin_bit_cast(float, x << 16); }
__device__ __forceinline__ float bfhi(unsigned x) { return __builtin_bit_cast(float, x & 0xffff0000u); }
__device__ __forceinline__ unsigned cvtpk(float lo, float hi) {
  return __builtin_bit_cast(unsigned, __builtin_convertvector(f32x2{lo, hi}, bf16x2));
}
__device__ __forceinline__ float rbf(float x) { return bflo(cvtpk(x, x)); }
__device__ __forceinline__ float asf(unsigned x) { return __builtin_bit_cast(float, x); }
__device__ __forceinline__ unsigned asu(float x) { return __builtin_bit_cast(unsigned, x); }
template <int CTRL> __device__ __forceinline__ float dpp(float x) {
  return __builtin_bit_cast(float, __builtin_amdgcn_mov_dpp(__builtin_bit_cast(int, x), CTRL, 0xf, 0xf, true));
}
template <int CTRL> __device__ __forceinline__ unsigned dppu(unsigned x) { return (unsigned)__builtin_amdgcn_mov_dpp((int)x, CTRL, 0xf, 0xf, true); }
constexpr int XOR1 = 0xB1, XOR2 = 0x4E, XOR7 = 0x141, ROR8 = 0x128, ROR4 = 0x124, SHL4 = 0x104, NBC = 0x150;
__device__ __forceinline__ float fold32(float a, float b) {
  auto s = __builtin_amdgcn_permlane32_swap(asu(a), asu(b), false, false); return asf(s[0]) + asf(s[1]);
}
__device__ __forceinline__ float fold16(float a, float b) {
  auto s = __builtin_amdgcn_permlane16_swap(asu(a), asu(b), false, false); return asf(s[0]) + asf(s[1]);
}
__device__ __forceinline__ float xrow_max(float x) {
  auto s = __builtin_amdgcn_permlane16_swap(asu(x), asu(x), false, false);
  x = fmaxf(asf(s[0]), asf(s[1]));
  auto t = __builtin_amdgcn_permlane32_swap(asu(x), asu(x), false, false);
  return fmaxf(asf(t[0]), asf(t[1]));
}
__device__ __forceinline__ float xrow_sum(float x) { x = fold16(x, x); return fold32(x, x); }
__device__ __forceinline__ float row_sum(float v) { v += dpp<XOR1>(v); v += dpp<XOR2>(v); v += dpp<ROR4>(v); v += dpp<ROR8>(v); return v; }
__device__ __forceinline__ float wave_sum(float t) { return xrow_sum(row_sum(t)); }
__device__ __forceinline__ void bar() {
  __builtin_amdgcn_sched_barrier(0);
  asm volatile("s_waitcnt lgkmcnt(0)\n\ts_barrier" ::: "memory");
  __builtin_amdgcn_sched_barrier(0);
}
__device__ __forceinline__ u32x4 ld_sc1(rsrc_t rs, unsigned off) { return __builtin_amdgcn_raw_buffer_load_b128(rs, off, 0, AUX_POLL); }
__device__ __forceinline__ unsigned umax3(unsigned a, unsigned b, unsigned c) { return max(max(a, b), c); }
__device__ __forceinline__ unsigned top(u32x4 v) { return max(umax3(v[0], v[1], v[2]), v[3]); }
__device__ __forceinline__ bool filled(u32x4 v) { return top(v) != EMPTY; }
constexpr unsigned OOB = 0x7ffffff0u;
template <int N> __device__ __forceinline__ void keep(const u32x4 (&v)[N]) {
#pragma unroll
  for (int i = 0; i < N; ++i) asm volatile("" :: "v"(v[i]));
}
__device__ __forceinline__ unsigned nz(unsigned v) { return v < 0xFFFFFFFEu ? v : 0xFFFFFFFEu; }
__device__ __forceinline__ bool wave_all(bool ok) { return __builtin_amdgcn_ballot_w64(!ok) == 0; }
__device__ __forceinline__ bool again(unsigned largest, unsigned& n) {
  n -= 1;
  return __builtin_amdgcn_ballot_w64(largest > (n != 0 ? EMPTY - 1 : EMPTY)) != 0;
}
__device__ __forceinline__ f32x4 mfma(u32x4 a, u32x4 b, f32x4 c) {
  return __builtin_amdgcn_mfma_f32_16x16x32_bf16(__builtin_bit_cast(bf16x8, a), __builtin_bit_cast(bf16x8, b), c, 0, 0, 0);
}
template <int PITCH = 32> __device__ __forceinline__ u32x4 frag_tr(LAS unsigned char* img, int s, int lane) {
  const int q = (lane >> 2) & 3, p = lane & 3, g = lane >> 4;
  LAS unsigned char* a = img + (32 * s + 8 * g + q) * PITCH + 8 * p;
  const s16x4 lo = __builtin_amdgcn_ds_read_tr16_b64_v4i16((LAS s16x4*)a);
  const s16x4 hi = __builtin_amdgcn_ds_read_tr16_b64_v4i16((LAS s16x4*)(a + 4 * PITCH));
  const u32x2 l2 = __builtin_bit_cast(u32x2, lo), h2 = __builtin_bit_cast(u32x2, hi);
  return u32x4{l2[0], l2[1], h2[0], h2[1]};
}

template <int I> struct Ic { static constexpr int value = I; };
template <int N, int I = 0, class F> __device__ __forceinline__ void static_for(F&& f) {
  if constexpr (I < N) { f(Ic<I>{}); static_for<N, I + 1>(f); }
}
#define DOT_BC(acc, w, lane) "v_dot2c_f32_bf16_dpp %" #acc ", %4, %" #w " row_newbcast:%" #lane " row_mask:0xf bank_mask:0xf\n\t"
#define DOT_BC4(w0, w1, w2, w3, lane) DOT_BC(0, w0, lane) DOT_BC(1, w1, lane) DOT_BC(2, w2, lane) DOT_BC(3, w3, lane)
#define DOT_BC16 DOT_BC4(5, 6, 7, 8, 21) DOT_BC4(9, 10, 11, 12, 22) DOT_BC4(13, 14, 15, 16, 23) DOT_BC4(17, 18, 19, 20, 24)
#define DOT_OPS : "+v"(y[0]), "+v"(y[1]), "+v"(y[2]), "+v"(y[3]) \
               : "v"(pk), "v"(wa[0]), "v"(wa[1]), "v"(wa[2]), "v"(wa[3]), "v"(wb[0]), "v"(wb[1]), "v"(wb[2]), "v"(wb[3]), \
                 "v"(wc[0]), "v"(wc[1]), "v"(wc[2]), "v"(wc[3]), "v"(wd[0]), "v"(wd[1]), "v"(wd[2]), "v"(wd[3]), \
                 "i"(I), "i"(I + 1), "i"(I + 2), "i"(I + 3)
template <int I> __device__ __forceinline__ void dot2_bcast(float (&y)[4], unsigned pk, u32x4 wa, u32x4 wb, u32x4 wc, u32x4 wd) {
  if constexpr (I == 0) asm volatile("s_nop 1\n\t" DOT_BC16 DOT_OPS);
  else if constexpr (I == 12) asm volatile(DOT_BC16 "s_nop 2" DOT_OPS);
  else asm volatile(DOT_BC16 DOT_OPS);
}
__device__ __forceinline__ void pin(u32x4& v) { asm volatile("" : "+v"(v)); }
template <int N> __device__ __forceinline__ void wait_vm() { asm volatile("s_waitcnt vmcnt(%0)" ::"i"(N) : "memory"); }
template <int I, int HD_> __device__ __forceinline__ void wait_paced(unsigned mask) {
  asm volatile("s_waitcnt vmcnt(%1)\n\ts_bitcmp1_b32 %0, %2\n\ts_cbranch_scc0 1f\n\ts_waitcnt vmcnt(%3)\n\ts_bitcmp1_b32 %0, %4\n\ts_cbranch_scc0 1f\n\ts_waitcnt vmcnt(%5)\n1:"
               :: "s"(mask), "i"(HD_ - 1), "i"(I), "i"(HD_ - 2), "i"(I + 8), "i"(HD_ - 3) : "memory", "scc");
}
__device__ __forceinline__ unsigned pace_mask(int q, int hd) {
  const int back = 8 * hd - q;
  return back <= 8 ? (1u << back) - 1u : 0xFFu | (((1u << (back - 8)) - 1u) << 8);
}

__global__ void __launch_bounds__(NTHR, 2)
fused_block(const u16* __restrict__ x, const u16* __restrict__ wq, unsigned char* ws, const u16* __restrict__ kc, const u16* __restrict__ vc,
            const u16* __restrict__ wo, const u16* __restrict__ wg, const u16* __restrict__ wu, const u16* __restrict__ wd, u16* __restrict__ out) {
  __shared__ __attribute__((aligned(16))) unsigned char smem[L_BYTES];
  LAS unsigned char* L = (LAS unsigned char*)smem;
  const int tid = threadIdx.x, lane = tid & 63, w = __builtin_amdgcn_readfirstlane(tid >> 6), r = lane >> 4, c = lane & 15;
  const int blk = blockIdx.x, xl = blk & 7, slot = blk >> 3;
  const int h = (slot >> 3) * 8 + xl, b = (slot & 7) >> 1, half = slot & 1, bh = b * NH + h;
  const int s12 = slot - 12, rj1 = xl * 8 + slot, rj2 = 4 * (xl + 8 * (s12 >> 2)) + (s12 & 3), rj3 = xl * 4 + slot - 8;
  const bool is1 = slot < 8, is2 = ((unsigned)s12 < 12u) & (rj2 < FF / 16), is3 = (slot >> 2) == 2;
  const int role23 = is2 ? 2 : 0, role13 = is3 ? 3 : role23, role = is1 ? 1 : role13;
  const int rj23 = is3 ? rj3 : rj2, rj = is1 ? rj1 : rj23;
  asm volatile("" :: "s"(x), "s"(wq), "s"(ws), "s"(kc), "s"(vc), "s"(wo), "s"(wg), "s"(wu), "s"(wd), "s"(out));
  const rsrc_t rsW = __builtin_amdgcn_make_buffer_rsrc((void*)ws, 0, WS_BYTES, RSRC3);
  u64 tag = 0;
  float y[4] = {0.f, 0.f, 0.f, 0.f};
  auto y_phase = [&](const u32x4 (&wz)[16], unsigned oy, unsigned olr) {
    f32x2 p[NS];
#pragma unroll
    for (int ww = 0; ww < NS; ++ww) p[ww] = *(LAS f32x2*)(L + olr + ww * DH * 4);
    const f32x2 s = (p[0] + p[1]) + (p[2] + p[3]);
    const unsigned pk = cvtpk(s[0], s[1]);
    dot2_bcast<0>(y, pk, wz[0], wz[1], wz[2], wz[3]);
    dot2_bcast<4>(y, pk, wz[4], wz[5], wz[6], wz[7]);
    dot2_bcast<8>(y, pk, wz[8], wz[9], wz[10], wz[11]);
    dot2_bcast<12>(y, pk, wz[12], wz[13], wz[14], wz[15]);
    const float v = fold16(fold32(y[0], y[2]), fold32(y[1], y[3]));
    __builtin_amdgcn_raw_buffer_store_b32(nz(asu(v)), rsW, oy, 0, AUX_SC1);
  };
  auto y_addr = [&](int B, int s) { return WS_Y + ((b * 16 + 4 * B + 2 * s + (r >> 1)) * 64 + 2 * h + half) * 128 + (r & 1) * 64 + c * 4; };
  const int b1 = rj >> 4, cg = rj & 15;
  unsigned xv1 = 0;

  if (w < NS) {
    const size_t kvoff = ((size_t)bh * SK + (size_t)half * (SK / 2)) * DH;
    const rsrc_t rsK = __builtin_amdgcn_make_buffer_rsrc((void*)(kc + kvoff), 0, (SK / 2) * DH * 2, RSRC3);
    const rsrc_t rsV = __builtin_amdgcn_make_buffer_rsrc((void*)(vc + kvoff), 0, (SK / 2) * DH * 2, RSRC3);
    const unsigned kvo = (unsigned)(w * 4 + r) * 256u + (unsigned)c * 16u;
    const int rot = (xl & 4) ? 32 : 0;
    auto toff = [&](int t) { return (unsigned)(t ^ rot) * TILE_BYTES; };
    u32x4 ring[PRE];
    auto issue = [&](u32x4& T, int j) {
      T = __builtin_amdgcn_raw_buffer_load_b128(rsK, kvo, toff(j), AUX_NT);
      __builtin_amdgcn_sched_barrier(0);
    };
    auto issueV = [&](u32x4& T, int t) {
      T = __builtin_amdgcn_raw_buffer_load_b128(rsV, kvo, toff(t), AUX_NT);
      __builtin_amdgcn_sched_barrier(0);
    };
    bar();
#pragma unroll
    for (int j = 0; j < PRE; ++j) {
      if (j >= DEPTH0) wait_vm<DEPTH0 - 1>();
      issue(ring[j], j);
    }
    bar();
    u32x4 qp;
    {
      f32x4 a = *(LAS f32x4*)(L + L_Q + c * 32), b4 = *(LAS f32x4*)(L + L_Q + c * 32 + 16);
#pragma unroll
      for (int j = 1; j < 4; ++j) { a += *(LAS f32x4*)(L + L_Q + j * 512 + c * 32); b4 += *(LAS f32x4*)(L + L_Q + j * 512 + c * 32 + 16); }
      qp = u32x4{cvtpk(a[0], a[1]), cvtpk(a[2], a[3]), cvtpk(b4[0], b4[1]), cvtpk(b4[2], b4[3])};
    }
    constexpr float C2 = 0.088388347648318440f * 1.4426950408889634f;
    LAS unsigned char* srow = L + L_S + (w * 4 + r) * S_ROW;
    float m = -INFINITY, sv = 0.f;
    auto score = [&](auto I, const u32x4& T) {
      float d = 0.f;
#pragma unroll
      for (int k = 0; k < 4; ++k) d = dot2(T[k], qp[k], d);
      d = row_sum(d);
      sv = (c & 7) == decltype(I)::value ? d : sv;
      asm volatile("" : "+v"(sv));
      __builtin_amdgcn_sched_barrier(0);
    };
    auto put_scores = [&](int t0) {
      const float s = sv * C2;
      m = fmaxf(m, s);
      *(LAS float*)(srow + (t0 + (c & 7)) * 4) = s;
    };
    static_for<PRE / RING>([&](auto G) {
      constexpr int g = decltype(G)::value;
      static_for<RING>([&](auto I) {
        constexpr int i = decltype(I)::value, j = g * RING + i;
        score(I, ring[j]);
        if (j < HD) issue(ring[j], PRE + j);
      });
      put_scores(g * RING);
    });
    unsigned mask = pace_mask(CFG_Q0, HD), mnext = mask;
    auto report = [&](int done) { *(volatile LAS int*)(L + L_PROG + w * 4) = done; };
    auto listen = [&]() { mnext = *(volatile LAS unsigned*)(L + L_MASK + w * 4); };
    auto adopt = [&]() { mask = (unsigned)__builtin_amdgcn_readfirstlane((int)mnext); };
#pragma unroll 1
    for (int t = PRE; t < NT - RING; t += RING) {
      static_for<RING>([&](auto I) {
        constexpr int i = decltype(I)::value;
        wait_paced<i, HD>(mask);
        issue(ring[(i + HD) % RING], t + i + HD);
        if (i == 0) report(t);
        if (i == 2) listen();
        score(I, ring[i]);
      });
      put_scores(t);
      adopt();
    }
    static_for<RING>([&](auto I) {
      constexpr int i = decltype(I)::value;
      wait_paced<i, HD>(mask);
      if (i + HD < RING) issue(ring[(i + HD) % RING], NT - RING + i + HD); else issueV(ring[(i + HD) % RING], i + HD - RING);
      score(I, ring[i]);
    });
    put_scores(NT - RING);
    m = fmaxf(m, dpp<XOR1>(m)); m = fmaxf(m, dpp<XOR2>(m)); m = fmaxf(m, dpp<ROR4>(m));
    f32x2 acc[4] = {};
    unsigned lsev = EMPTY;
    f32x4 sa = *(LAS f32x4*)srow, sb = *(LAS f32x4*)(srow + 16);
    auto pv = [&](float s, const u32x4& T) {
      const float p = __builtin_amdgcn_exp2f(s - m);
#pragma unroll
      for (int k = 0; k < 4; ++k) acc[k] = __builtin_elementwise_fma(f32x2{p, p}, f32x2{bflo(T[k]), bfhi(T[k])}, acc[k]);
      asm volatile("" : "+v"(acc[0]), "+v"(acc[1]), "+v"(acc[2]), "+v"(acc[3]));
      __builtin_amdgcn_sched_barrier(0);
    };
#pragma unroll 1
    for (int t = 0; t < NT - FT; t += RING) {
      static_for<RING>([&](auto I) {
        constexpr int i = decltype(I)::value;
        wait_paced<i, HD>(mask);
        issueV(ring[(i + HD) % RING], t + i + HD);
        if (i == 0) report(NT + t);
        if (i == 1) listen();
        if (i == 5) lsev = *(volatile LAS unsigned*)(L + L_ST);
        pv(i < 4 ? sa[i & 3] : sb[i & 3], ring[i]);
        if (i == 3) sa = *(LAS f32x4*)(srow + (t + RING) * 4);
        if (i == 7) sb = *(LAS f32x4*)(srow + (t + RING) * 4 + 16);
      });
      adopt();
    }
    {
      for (unsigned spin = 0; spin < SPIN_CAP; ++spin) {
        if (wave_all(lsev != EMPTY)) break;
        __builtin_amdgcn_s_sleep(8);
        lsev = *(volatile LAS unsigned*)(L + L_ST);
      }
      const float lse = asf(lsev), fs = __builtin_amdgcn_exp2f(m - lse);
#pragma unroll
      for (int k = 0; k < 4; ++k) acc[k] *= fs;
      m = lse;
    }
    unsigned oy = y_addr(w, 1), ored = L_RED + (w * DH + 8 * c + 2 * r) * 4, olr = L_RED + lane * 8;
    asm volatile("" : "+v"(oy), "+v"(ored), "+v"(olr), "+v"(y[0]), "+v"(y[1]), "+v"(y[2]), "+v"(y[3]), "+v"(acc[0]), "+v"(acc[1]), "+v"(acc[2]), "+v"(acc[3]), "+v"(m));
    u32x4 wz[16];
    f32x4 sf[FT / 4];
    sf[0] = sa; sf[1] = sb;
#pragma unroll
    for (int i = 2; i < FT / 4; ++i) sf[i] = *(LAS f32x4*)(srow + (NT - FT + 4 * i) * 4);
    static_for<FT>([&](auto I) {
      constexpr int i = decltype(I)::value;
      static_for<FR>([&](auto J) {
        constexpr int n = HD + i * FR + decltype(J)::value;
        if constexpr (n < FT) issueV(ring[n], NT - FT + n);
      });
      if constexpr (i > CFG_WZ0 && i <= CFG_WZ0 + 16) pin(wz[i - CFG_WZ0 - 1]);
      if constexpr (i >= CFG_WZ0 && i < CFG_WZ0 + 16) wz[i - CFG_WZ0] = *(LAS u32x4*)(L + L_W + w * 16384 + (i - CFG_WZ0) * 1024 + lane * 16);
      pv(sf[i >> 2][i & 3], ring[i]);
    });
    float s4[4];
#pragma unroll
    for (int i = 0; i < 4; ++i) s4[i] = fold32(acc[i >> 1][i & 1], acc[2 + (i >> 1)][i & 1]);
    const f32x2 t2 = {fold16(s4[0], s4[2]), fold16(s4[1], s4[3])};
    *(LAS f32x2*)(L + ored) = t2;
    bar();
    y_phase(wz, oy, olr);
  } else {
    const int hw = w - NS, ht = tid - NS * 64;
    auto make_tag = [&](u32x2 e) {
      asm volatile("" : "+v"(e));
      const u64 ep = (u64)(unsigned)__builtin_amdgcn_readfirstlane((int)e[0]) | ((u64)(unsigned)__builtin_amdgcn_readfirstlane((int)e[1]) << 32);
      return (ep + 1) * 0x9E3779B97F4A7C15ull;
    };
    if (hw == 2) {
      const u32x4 e4 = {EMPTY, EMPTY, EMPTY, EMPTY};
      const unsigned o = WS_BOX + (blk * CLR + lane) * 16;
      __builtin_amdgcn_raw_buffer_store_b128(e4, rsW, o, 0, AUX_SC1);
      __builtin_amdgcn_raw_buffer_store_b128(e4, rsW, o + 1024, 0, AUX_SC1);
      if (lane < CLR - 128) __builtin_amdgcn_raw_buffer_store_b128(e4, rsW, o + 2048, 0, AUX_SC1);
    }
    if (hw == 3 && lane < 4) {
      *(LAS int*)(L + L_PROG + lane * 4) = 0; *(LAS unsigned*)(L + L_MASK + lane * 4) = pace_mask(CFG_Q0, HD);
      *(LAS unsigned*)(L + L_ST + lane * 4) = lane == 0 ? EMPTY : 0; *(LAS int*)(L + L_ML + lane * 4) = 0;
    }
    {
      const u32x4 xr = *(const GAS u32x4*)((const GAS u16*)x + b * DM + 8 * lane);
      const u32x2 ep = __builtin_amdgcn_raw_buffer_load_b64(rsW, WS_EPOCH, 0, AUX_POLL);
      u32x4 wr[32];
      const rsrc_t rsQ = __builtin_amdgcn_make_buffer_rsrc((void*)wq, 0, DM * NH * DH * 2, RSRC3);
      const unsigned qo = (unsigned)(128 * hw + 32 * r) * (NH * DH * 2) + (unsigned)(h * DH + 8 * c) * 2;
#pragma unroll
      for (int u = 0; u < 32; ++u) wr[u] = __builtin_amdgcn_raw_buffer_load_b128(rsQ, qo, u * (NH * DH * 2), 0);
      __builtin_amdgcn_sched_barrier(0);
      bar();
      float v[8];
#pragma unroll
      for (int k = 0; k < 4; ++k) { v[2 * k] = bflo(xr[k]); v[2 * k + 1] = bfhi(xr[k]); }
      float s = 0.f;
#pragma unroll
      for (int k = 0; k < 8; ++k) s += v[k];
      const float mu = wave_sum(s) * (1.f / DM);
      float ss = 0.f;
#pragma unroll
      for (int k = 0; k < 8; ++k) { v[k] -= mu; ss += v[k] * v[k]; }
      const float rs = 1.f / sqrtf(wave_sum(ss) * (1.f / DM) + 1e-5f);
      u32x4 xn;
#pragma unroll
      for (int k = 0; k < 4; ++k) xn[k] = cvtpk(v[2 * k] * rs, v[2 * k + 1] * rs);
      *(LAS u32x4*)(L + L_XN + hw * 1024 + lane * 16) = xn;
      u32x4 xq[4];
#pragma unroll
      for (int j = 0; j < 4; ++j) xq[j] = *(LAS u32x4*)(L + L_XN + hw * 1024 + (128 * hw + 32 * r) * 2 + 16 * j);
      float qa[8] = {};
#pragma unroll
      for (int i = 0; i < 16; ++i)
#pragma unroll
        for (int m4 = 0; m4 < 4; ++m4) {
          qa[2 * m4] = dot2(__builtin_amdgcn_perm(wr[2 * i + 1][m4], wr[2 * i][m4], 0x05040100u), xq[i >> 2][i & 3], qa[2 * m4]);
          qa[2 * m4 + 1] = dot2(__builtin_amdgcn_perm(wr[2 * i + 1][m4], wr[2 * i][m4], 0x07060302u), xq[i >> 2][i & 3], qa[2 * m4 + 1]);
        }
      float s4[4];
#pragma unroll
      for (int i = 0; i < 4; ++i) s4[i] = fold32(qa[i], qa[i + 4]);
      *(LAS f32x2*)(L + L_Q + (hw * DH + 8 * c + 2 * r) * 4) = f32x2{fold16(s4[0], s4[2]), fold16(s4[1], s4[3])};
      tag = make_tag(ep);
    }
    bar();
    if (hw == 2) {
      wait_vm<0>();
      if (lane == 0) __hip_atomic_store((GAS u64*)(ws + WS_FLAGS) + blk, tag, __ATOMIC_RELAXED, __HIP_MEMORY_SCOPE_AGENT);
    }
    const unsigned t0 = (unsigned)tag, t1 = (unsigned)(tag >> 32);
    xv1 = __builtin_amdgcn_raw_buffer_load_b32(__builtin_amdgcn_make_buffer_rsrc((void*)x, 0, NB * DM * 2, RSRC3),
                                               role == 1 && hw == 0 ? (b1 * DM + 128 * (cg >> 2) + 8 * c + 2 * (cg & 3)) * 2 : OOB, 0, 0);
    u32x4 wz[16];
    {
      const rsrc_t rsO = __builtin_amdgcn_make_buffer_rsrc((void*)wo, 0, NH * DH * DM * 2, RSRC3);
      const unsigned oo = (unsigned)((h * DH + 32 * r) * DM + 128 * hw + 8 * c) * 2;
#pragma unroll
      for (int g = 0; g < 8; ++g) {
        u32x4 wv[4];
#pragma unroll
        for (int j = 0; j < 4; ++j) wv[j] = __builtin_amdgcn_raw_buffer_load_b128(rsO, oo, (4 * g + j) * DM * 2, 0);
#pragma unroll
        for (int i = 0; i < 2; ++i) {
          unsigned pp[8];
#pragma unroll
          for (int m4 = 0; m4 < 4; ++m4) {
            pp[2 * m4] = __builtin_amdgcn_perm(wv[2 * i + 1][m4], wv[2 * i][m4], 0x05040100u);
            pp[2 * m4 + 1] = __builtin_amdgcn_perm(wv[2 * i + 1][m4], wv[2 * i][m4], 0x07060302u);
          }
          wz[2 * g + i] = u32x4{pp[0], pp[1], pp[2], pp[3]};
          *(LAS u32x4*)(L + L_W + hw * 16384 + (2 * g + i) * 1024 + lane * 16) = u32x4{pp[4], pp[5], pp[6], pp[7]};
          asm volatile("" : "+v"(wz[2 * g + i]) :: "memory");
        }
        __builtin_amdgcn_sched_barrier(0);
      }
      if (lane == 0) *(volatile LAS int*)(L + L_ML + hw * 4) = 1;
    }
    if (hw == 3) {
      for (unsigned spin = 0; spin < SPIN_CAP; ++spin) {
        asm volatile("" ::: "memory");
        const u32x4 f0 = ld_sc1(rsW, WS_FLAGS + lane * 32), f1 = ld_sc1(rsW, WS_FLAGS + lane * 32 + 16);
        if (wave_all(f0[0] == t0 && f0[1] == t1 && f0[2] == t0 && f0[3] == t1 && f1[0] == t0 && f1[1] == t1 && f1[2] == t0 && f1[3] == t1)) break;
        __builtin_amdgcn_s_sleep(16);
      }
      if (lane == 0) *(volatile LAS int*)(L + L_ST + 8) = 1;
#pragma unroll 1
      for (int k = 0; k < CFG_NCK; ++k) {
        const int ck = CFG_C0 + k * CFG_CI;
        unsigned tw = 0, now = 0;
        bool got = false;
        for (unsigned spin = 0; spin < SPIN_CAP; ++spin) {
          const int pr = *(volatile LAS int*)(L + L_PROG + (lane & 3) * 4);
          now = (unsigned)__builtin_amdgcn_s_memrealtime();
          tw = got ? tw : now;
          got = got || pr >= ck;
          if (wave_all(got)) break;
          __builtin_amdgcn_s_sleep(3);
        }
        const unsigned base = (unsigned)__builtin_amdgcn_readfirstlane((int)tw);
        float dw = (float)(int)(tw - base), dg = dw;
        dg += dpp<XOR1>(dg); dg += dpp<XOR2>(dg);
        const unsigned tg = base + (unsigned)(int)(dg * 0.25f);
        dw -= (float)(int)(tg - base);
        if (lane == 0 && slot == 31) __builtin_amdgcn_raw_buffer_store_b32(nz(tg), rsW, WS_PACE + (k * 8 + xl) * 128, 0, AUX_SC1);
        for (unsigned spin = 0; spin < SPIN_CAP; ++spin) {
          now = (unsigned)__builtin_amdgcn_s_memrealtime();
          if ((int)(now - tg) >= CFG_DL) break;
          __builtin_amdgcn_s_sleep(8);
        }
        const bool mine = lane < 8;
        const unsigned e = __builtin_amdgcn_raw_buffer_load_b32(rsW, mine ? WS_PACE + (k * 8 + lane) * 128 : OOB, 0, AUX_POLL);
        const float d = (float)(int)((e == EMPTY ? now : e) - tg);
        const float lag = dw - wave_sum(mine ? d : 0.f) * 0.125f + (role == 2 ? CFG_RB : role == 3 ? CFG_RB3 : 0);
        const int q = min(max(CFG_Q0 + (int)__builtin_rintf(lag * (1.f / CFG_HZ)), CFG_QLO), CFG_QHI);
        if (lane < 4) *(volatile LAS unsigned*)(L + L_MASK + lane * 4) = pace_mask(q, HD);
      }
    }
    if (hw == 2) {
      for (unsigned spin = 0; spin < SPIN_CAP; ++spin) {
        const int pr = *(volatile LAS int*)(L + L_PROG + (lane & 3) * 4), ok = *(volatile LAS int*)(L + L_ST + 8);
        const int okw = *(volatile LAS int*)(L + L_ML + (lane & 3) * 4);
        if (wave_all(pr >= NT && ok != 0 && okw != 0)) break;
        __builtin_amdgcn_s_sleep(32);
      }
      asm volatile("" ::: "memory");
      LAS unsigned char* sr = L + L_S + (lane >> 2) * S_ROW + (lane & 3) * (NT / 4 * 4);
      float mh = -INFINITY;
#pragma unroll 4
      for (int j = 0; j < NT / 16; ++j) {
        const f32x4 v = *(LAS f32x4*)(sr + j * 16);
        mh = fmaxf(fmaxf(mh, fmaxf(v[0], v[1])), fmaxf(v[2], v[3]));
      }
      mh = fmaxf(mh, dpp<XOR1>(mh)); mh = fmaxf(mh, dpp<XOR2>(mh)); mh = fmaxf(mh, dpp<ROR4>(mh)); mh = fmaxf(mh, dpp<ROR8>(mh));
      mh = xrow_max(mh);
      float lh = 0.f;
#pragma unroll 4
      for (int j = 0; j < NT / 16; ++j) {
        const f32x4 v = *(LAS f32x4*)(sr + j * 16);
#pragma unroll
        for (int k = 0; k < 4; ++k) lh += __builtin_amdgcn_exp2f(v[k] - mh);
      }
      lh = wave_sum(lh);
      if (lane == 0) __builtin_amdgcn_raw_buffer_store_b64(u32x2{nz(asu(mh)), nz(asu(lh))}, rsW, WS_ML + blk * 128, 0, AUX_SC1);
      u32x2 o;
      for (unsigned spin = 0; spin < SPIN_CAP; ++spin) {
        asm volatile("" ::: "memory");
        o = __builtin_amdgcn_raw_buffer_load_b64(rsW, lane == 0 ? WS_ML + (blk ^ 8) * 128 : OOB, 0, AUX_POLL);
        if (__builtin_amdgcn_ballot_w64(lane == 0 && o[0] != EMPTY && o[1] != EMPTY) != 0) break;
        __builtin_amdgcn_s_sleep(64);
      }
      const float mo = asf(o[0]), lo = asf(o[1]), mt = fmaxf(mh, mo);
      const float lt = fmaf(__builtin_amdgcn_exp2f(mh - mt), lh, __builtin_amdgcn_exp2f(mo - mt) * lo);
      if (lane == 0) *(volatile LAS unsigned*)(L + L_ST) = nz(asu(mt + __builtin_amdgcn_logf(lt)));
    }
    unsigned oy = y_addr(hw, 0), olr = L_RED + lane * 8;
    asm volatile("" : "+v"(oy), "+v"(olr), "+v"(y[0]), "+v"(y[1]), "+v"(y[2]), "+v"(y[3]));
    bar();
    y_phase(wz, oy, olr);
    if (role >= 2) {
      __builtin_amdgcn_sched_barrier(0);
      const rsrc_t ra = __builtin_amdgcn_make_buffer_rsrc((void*)(role == 2 ? wg : wd), 0, DM * FF * 2, RSRC3);
      const rsrc_t rb = __builtin_amdgcn_make_buffer_rsrc((void*)(role == 2 ? wu : wd), 0, DM * FF * 2, RSRC3);
      const int p = ht >> 1, row = role == 2 ? 8 * ((p >> 1) & 15) + 2 * (p >> 5) + (p & 1) : p;
      const unsigned pitch = role == 2 ? FF * 2 : DM * 2, o0 = (unsigned)row * pitch + 32 * rj + 16 * (ht & 1);
      u32x4 rw[12];
#pragma unroll
      for (int n = 0; n < 12; ++n) {
        const unsigned so = role == 2 ? (n & 3) * 128 * FF * 2 : n * 128 * DM * 2;
        rw[n] = __builtin_amdgcn_raw_buffer_load_b128((n >> 2) == 1 ? rb : ra, n >= 8 && role == 2 ? OOB : o0 + so, 0, 0);
      }
      __builtin_amdgcn_sched_barrier(0);
#pragma unroll
      for (int n = 0; n < 12; ++n) *(LAS u32x4*)(L + L_W + n * 4096 + ht * 16) = rw[n];
    }
  }
  if (role >= 2) bar();
  const unsigned nowhere = L_S + w * 2048 + lane * 16;

  if (role == 1) {
    const int j = lane & 7;
    unsigned oy = WS_Y + rj * 8192 + tid * 16, ol = L_R2 + (w * 32 + 8 * (j & 3) + 2 * r + (j >> 2)) * 4;
    unsigned oh = WS_H + (4 * r + (c & 3)) * H_BYTES + b1 * 1024 + cg * 64 + (c >> 2) * 16, oc = L_R2 + 8 * c;
    float x0 = bflo(xv1), x1 = bfhi(xv1);
    asm volatile("" : "+v"(oy), "+v"(ol), "+v"(oh), "+v"(oc), "+v"(x0), "+v"(x1));
    u32x4 yv;
    unsigned n = SPIN_CAP;
#pragma unroll 1
    do {
      __builtin_amdgcn_s_sleep(1);
      asm volatile("" ::: "memory");
      yv = ld_sc1(rsW, oy);
    } while (again(top(yv), n));
    float y0 = asf(yv[0]), y1 = asf(yv[1]), y2 = asf(yv[2]), y3 = asf(yv[3]);
    asm volatile("" : "+v"(y0), "+v"(y1), "+v"(y2), "+v"(y3));
    float s = fold16(fold32(y0, y2), fold32(y1, y3));
    s += dpp<ROR8>(s);
    *(LAS float*)(L + ol) = s;
    bar();
    if (w == NS) {
      f32x2 p[NW];
#pragma unroll
      for (int ww = 0; ww < NW; ++ww) p[ww] = *(LAS f32x2*)(L + oc + ww * 128);
      const f32x2 a = ((p[0] + p[1]) + (p[2] + p[3])) + ((p[4] + p[5]) + (p[6] + p[7]));
      const unsigned at = cvtpk(a[0], a[1]);
      const unsigned hp = nz(cvtpk(x0 + bflo(at), x1 + bfhi(at)));
      const u32x4 o = {dppu<0x00>(hp), dppu<0x55>(hp), dppu<0xAA>(hp), dppu<0xFF>(hp)};
      __builtin_amdgcn_raw_buffer_store_b128(o, rsW, oh, 0, AUX_SC1);
    }
  } else if (role == 2) {
    u32x4 fg[2], fu[2];
#pragma unroll
    for (int s = 0; s < 2; ++s) { fg[s] = frag_tr(L + L_W, 2 * w + s, lane); fu[s] = frag_tr(L + L_W + 16384, 2 * w + s, lane); }
    unsigned oh = WS_H + ((slot * 8 + xl) % HREP) * H_BYTES + (c & 3) * 1024 + (2 * w + ((c >> 2) & 1)) * 64 + 16 * r;
    float csg = 0.f, csu = 0.f;
    {
      const u32x4 ones = {0x3F803F80u, 0x3F803F80u, 0x3F803F80u, 0x3F803F80u};
      f32x4 ag = {0.f, 0.f, 0.f, 0.f}, au = {0.f, 0.f, 0.f, 0.f};
#pragma unroll
      for (int s = 0; s < 2; ++s) { ag = mfma(fg[s], ones, ag); au = mfma(fu[s], ones, au); }
      if (c == 0) {
        *(LAS f32x4*)(L + L_A + (w * 8 + r) * 16) = ag;
        *(LAS f32x4*)(L + L_A + (w * 8 + 4 + r) * 16) = au;
      }
      bar();
      if (w == 0) {
#pragma unroll
        for (int ww = 0; ww < NW; ++ww) { csg += *(LAS float*)(L + L_A + (ww * 32 + c) * 4); csu += *(LAS float*)(L + L_A + (ww * 32 + 16 + c) * 4); }
      }
    }
    unsigned ol = c < NB ? L_R2 + (w * 128 + c * 16 + 4 * r) * 4 : nowhere, os = c < NB ? L_Q + ((c * 2 + (r >> 1)) * 8 + w) * 4 : nowhere;
    unsigned oc = L_R2 + lane * 4, oq = L_Q + lane * 4, oa = (c & 4) == 0 ? WS_ACT + (c & 3) * ACT_BYTES + rj * 128 + (c >> 3) * 64 + r * 16 : OOB;
    float s1 = 0.f, s2 = 0.f;
    asm volatile("" : "+v"(oh), "+v"(ol), "+v"(os), "+v"(oc), "+v"(oq), "+v"(oa), "+v"(csg), "+v"(csu), "+v"(s1), "+v"(s2));
    u32x4 hv;
    unsigned n = SPIN_CAP;
#pragma unroll 1
    do {
      __builtin_amdgcn_s_sleep(1);
      asm volatile("" ::: "memory");
      hv = ld_sc1(rsW, oh);
    } while (again(top(hv), n));
    {
      const f32x4 z = {0.f, 0.f, 0.f, 0.f};
      f32x4 ag = mfma(fg[0], hv, z), au = mfma(fu[0], hv, z);
      const u32x4 hs = {dppu<SHL4>(hv[0]), dppu<SHL4>(hv[1]), dppu<SHL4>(hv[2]), dppu<SHL4>(hv[3])};
      ag = mfma(fg[1], hs, ag); au = mfma(fu[1], hs, au);
#pragma unroll
      for (int k = 0; k < 4; ++k) { s1 = dot2(hv[k], 0x3F803F80u, s1); s2 = dot2(hv[k], hv[k], s2); }
      float st = fold32(s1, s2);
      st = fold16(st, st);
      st += dpp<SHL4>(st);
      *(LAS float*)(L + os) = st;
      *(LAS f32x4*)(L + ol) = ag;
      *(LAS f32x4*)(L + ol + 256) = au;
    }
    bar();
    if (w == 0) {
      f32x2 gu[NW];
#pragma unroll
      for (int ww = 0; ww < NW; ++ww) gu[ww] = f32x2{*(LAS float*)(L + oc + ww * 512), *(LAS float*)(L + oc + ww * 512 + 256)};
      float sv = *(LAS float*)(L + oq);
      sv += dpp<XOR1>(sv); sv += dpp<XOR2>(sv); sv += dpp<XOR7>(sv);
      const float ov = dpp<ROR8>(sv);
      const float mu = (c < 8 ? sv : ov) * (1.f / DM);
      const float var = fmaxf(fmaf(-mu, mu, (c < 8 ? ov : sv) * (1.f / DM)), 0.f);
      const float rs = __builtin_amdgcn_rsqf(var + 1e-5f);
      const f32x2 sg = ((gu[0] + gu[1]) + (gu[2] + gu[3])) + ((gu[4] + gu[5]) + (gu[6] + gu[7]));
      const float g = rbf(fmaf(-mu, csg, sg[0]) * rs);
      constexpr float K1 = -2.f * 1.4426950408889634f * 0.7978845608028654f, K3 = K1 * 0.044715f;
      const float ge = rbf(g * __builtin_amdgcn_rcpf(1.f + __builtin_amdgcn_exp2f(g * fmaf(g * g, K3, K1))));
      const float a = ge * rbf(fmaf(-mu, csu, sg[1]) * rs);
      const unsigned p0 = nz(cvtpk(dpp<0x00>(a), dpp<0x55>(a))), p1 = nz(cvtpk(dpp<0xAA>(a), dpp<0xFF>(a)));
      const u32x4 o = {p0, p1, dppu<XOR7>(p0), dppu<XOR7>(p1)};
      __builtin_amdgcn_raw_buffer_store_b128(o, rsW, oa, 0, AUX_SC1);
    }
  } else if (role == 3) {
    u32x4 fd[6];
#pragma unroll
    for (int i = 0; i < 6; ++i) fd[i] = frag_tr(L + L_W, (w + 8 * i < FF / 32) ? w + 8 * i : 0, lane);
    const rsrc_t rsO = __builtin_amdgcn_make_buffer_rsrc((void*)out, 0, NB * DM * 2, RSRC3);
    unsigned oh = WS_H + ((slot * 8 + xl) % HREP) * H_BYTES + r * 1024 + (rj >> 3) * 256 + ((c >> 1) & 3) * 64 + (2 * (rj & 7) + (c >> 3)) * 4;
    unsigned oa = WS_ACT + ((slot + w) % AREP) * ACT_BYTES + r * 64 + (lane & 3) * 16 + w * 256;
    unsigned ol = c < NB ? L_R2 + (w * 64 + c * 16 + 4 * r) * 4 : nowhere, oc = L_R2 + lane * 4;
    unsigned oo = (c & 1) == 0 ? (r * DM + 16 * rj + c) * 2 : OOB;
    const bool six = w + 40 < FF / 32;
    unsigned oa5 = six ? oa + 40 * 256 : oa;
    asm volatile("" : "+v"(oh), "+v"(oa), "+v"(oa5), "+v"(ol), "+v"(oc), "+v"(oo));
    unsigned hv, n = SPIN_CAP;
#pragma unroll 1
    do {
      __builtin_amdgcn_s_sleep(CFG_GSL);
      asm volatile("" ::: "memory");
      hv = __builtin_amdgcn_raw_buffer_load_b32(rsW, oh, 0, AUX_POLL);
    } while (again(hv, n));
    float hf = (c & 1) ? bfhi(hv) : bflo(hv);
    if (!six) fd[5] = u32x4{0u, 0u, 0u, 0u};
    asm volatile("" : "+v"(hf), "+v"(fd[0]), "+v"(fd[1]), "+v"(fd[2]), "+v"(fd[3]), "+v"(fd[4]), "+v"(fd[5]));
    u32x4 pa[6];
    unsigned t;
    n = SPIN_CAP;
#pragma unroll 1
    do {
      __builtin_amdgcn_s_sleep(1);
      asm volatile("" ::: "memory");
#pragma unroll
      for (int i = 0; i < 5; ++i) pa[i] = __builtin_amdgcn_raw_buffer_load_b128(rsW, oa, i * 8 * 256, AUX_POLL);
      pa[5] = ld_sc1(rsW, oa5);
      t = 0;
#pragma unroll
      for (int i = 0; i < 6; ++i) t = umax3(umax3(t, pa[i][0], pa[i][1]), pa[i][2], pa[i][3]);
    } while (again(t, n));
    f32x4 a0 = {0.f, 0.f, 0.f, 0.f}, a1 = {0.f, 0.f, 0.f, 0.f};
#pragma unroll
    for (int i = 0; i < 6; i += 2) { a0 = mfma(fd[i], pa[i], a0); a1 = mfma(fd[i + 1], pa[i + 1], a1); }
    *(LAS f32x4*)(L + ol) = a0 + a1;
    bar();
    if (w == 0) {
      f32x2 p[NW / 2];
#pragma unroll
      for (int ww = 0; ww < NW / 2; ++ww) p[ww] = f32x2{*(LAS float*)(L + oc + ww * 512), *(LAS float*)(L + oc + ww * 512 + 256)};
      const f32x2 s2 = (p[0] + p[1]) + (p[2] + p[3]);
      const float s = s2[0] + s2[1];
      const float o1 = hf + rbf(s);
      __builtin_amdgcn_raw_buffer_store_b32(cvtpk(dpp<0xA0>(o1), dpp<0xF5>(o1)), rsO, oo, 0, CFG_OUTAUX);
    }
    if (rj == 0 && tid == NS * 64) __hip_atomic_store((GAS u64*)(ws + WS_EPOCH), tag, __ATOMIC_RELAXED, __HIP_MEMORY_SCOPE_AGENT);
  }
}

}

extern "C" void kernel_launch(void* const* d_in, const int* in_sizes, int n_in, void* d_out, int out_size, void* d_ws, size_t ws_size, hipStream_t stream) {
  fused_block<<<NWG, NTHR, 0, stream>>>((const u16*)d_in[0], (const u16*)d_in[3], (unsigned char*)d_ws, (const u16*)d_in[1], (const u16*)d_in[2], (const u16*)d_in[4],
                                        (const u16*)d_in[6], (const u16*)d_in[7], (const u16*)d_in[8], (u16*)d_out);
}
```
